```python
import torch

def _softmax(x, axis=-1):
    return torch.softmax(x, dim=axis)
def _gelu(x):
    return torch.nn.functional.gelu(x, approximate="tanh")
def _silu(x):
    return torch.nn.functional.silu(x)
def _sigmoid(x):
    return torch.sigmoid(x)
def _layernorm(x, axis=-1, eps=1e-5):
    m = x.mean(dim=axis, keepdim=True)
    v = x.var(dim=axis, keepdim=True, unbiased=False)
    return (x - m) / torch.sqrt(v + eps)
def _scatter_add(tgt, idx, src, axis=0):
    return tgt.clone().index_add_(axis, idx, src)
def _segment_sum(src, seg, n):
    out = torch.zeros((n,) + src.shape[1:], dtype=src.dtype)
    return out.index_add_(0, seg, src)
def _pad(x, pw):
    flat = tuple(v for pair in reversed(pw) for v in pair)
    return torch.nn.functional.pad(x, flat)

kernel_name = 'recurrent_scan__ssm_conv_s5__glu_gated_bilinear__prompt'

def setup_inputs():

    _w = (lambda a: (lambda t: t.to(torch.bfloat16) if t.is_floating_point() else t)(torch.as_tensor(a)))
    def _A_setup():
        B, S, D = 1, 2048, 512
        P = D
        Lambda_re = -0.5 * (lambda _x: torch.exp(torch.as_tensor(_x)))((lambda *s: torch.randn(*s, dtype=torch.bfloat16) * 0.8)(P))
        Lambda_im = (lambda *s: torch.randn(*s, dtype=torch.bfloat16) * 0.8)(P) * 1.0
        B_re = (lambda *s: torch.randn(*s, dtype=torch.bfloat16) * 1.6)(P, D) * (D ** -0.5)
        B_im = (lambda *s: torch.randn(*s, dtype=torch.bfloat16) * 0.8)(P, D) * (D ** -0.5)
        C_re = (lambda *s: torch.randn(*s, dtype=torch.bfloat16) * 1.6)(D, P) * (P ** -0.5)
        C_im = (lambda *s: torch.randn(*s, dtype=torch.bfloat16) * 0.8)(D, P) * (P ** -0.5)
        log_dt = (lambda *s: torch.randn(*s, dtype=torch.bfloat16) * 0.8)(P) * 0.3 - 2.0
        Dskip = (lambda *s: torch.randn(*s, dtype=torch.bfloat16) * 0.8)(D)
        return {
            "x": _w((lambda *s: torch.randn(*s, dtype=torch.bfloat16) * 0.05)(B, S, D)),
            "Lambda_re": _w(Lambda_re),
            "Lambda_im": _w(Lambda_im),
            "B_re": _w(B_re),
            "B_im": _w(B_im),
            "C_re": _w(C_re),
            "C_im": _w(C_im),
            "log_dt": _w(log_dt),
            "Dskip": _w(Dskip),
        }
    _A_in = _A_setup()

    def _B_setup():
        B, S, D, F = 1, 2048, 512, 1792
        return {
            "x": _w((lambda *s: torch.randn(*s, dtype=torch.bfloat16) * 0.05)(B, S, D)),
            "w_gate": _w((lambda *s: torch.randn(*s, dtype=torch.bfloat16) * 0.2)(D, F) * (D ** -0.5)),
            "w_up": _w((lambda *s: torch.randn(*s, dtype=torch.bfloat16) * 0.2)(D, F) * (D ** -0.5)),
            "w_down": _w((lambda *s: torch.randn(*s, dtype=torch.bfloat16) * 0.2)(F, D) * (F ** -0.5)),
        }
    _B_in = _B_setup()

    return {
        "A_x": _A_in["x"],
        "A_Lambda_re": _A_in["Lambda_re"],
        "A_Lambda_im": _A_in["Lambda_im"],
        "A_B_re": _A_in["B_re"],
        "A_B_im": _A_in["B_im"],
        "A_C_re": _A_in["C_re"],
        "A_C_im": _A_in["C_im"],
        "A_log_dt": _A_in["log_dt"],
        "A_Dskip": _A_in["Dskip"],
        "B_x": _B_in["x"],
        "B_w_gate": _B_in["w_gate"],
        "B_w_up": _B_in["w_up"],
        "B_w_down": _B_in["w_down"],
    }

def reference(A_x, A_Lambda_re, A_Lambda_im, A_B_re, A_B_im, A_C_re, A_C_im, A_log_dt, A_Dskip, B_x, B_w_gate, B_w_up, B_w_down):
    def _A_ref(x, Lambda_re, Lambda_im, B_re, B_im, C_re, C_im, log_dt, Dskip):
        Bsz, L, H = x.shape
        Lam = Lambda_re + 1j * Lambda_im
        Bmat = B_re + 1j * B_im
        Cmat = C_re + 1j * C_im
        dt = torch.exp(log_dt)
        Lam_bar = torch.exp(dt * Lam)
        B_bar = ((Lam_bar - 1.0) / Lam)[:, None] * Bmat
        Bu = torch.einsum("bld,pd->blp", x + 0j, B_bar)
        h = torch.zeros_like(Bu[:, 0, :])
        hs = []
        for t in range(L):
            h = Lam_bar[None, :] * h + Bu[:, t, :]
            hs.append(h)
        hseq = torch.stack(hs, 1)
        y = 2.0 * torch.einsum("blp,dp->bld", hseq, Cmat).real + x * Dskip[None, None, :]
        return y

    def _B_ref(x, w_gate, w_up, w_down):
        g = torch.einsum("bsd,df->bsf", x, w_gate)
        u = torch.einsum("bsd,df->bsf", x, w_up)
        h = g * u
        return torch.einsum("bsf,fd->bsd", h, w_down)

    x = A_x
    xn = _layernorm(x, axis=-1)
    s = _A_ref(xn, A_Lambda_re, A_Lambda_im, A_B_re, A_B_im, A_C_re, A_C_im, A_log_dt, A_Dskip)
    g = _silu(_B_ref(xn, B_w_gate, B_w_up, B_w_down))
    return x + s * g
```

```cpp
#include <hip/hip_runtime.h>
#include <hip/hip_bf16.h>
#include <stdint.h>

typedef __attribute__((ext_vector_type(8))) short bf16x8;
typedef __attribute__((ext_vector_type(4))) float f32x4;
typedef unsigned short u16;

constexpr int SL = 2048;
constexpr int DM = 512;
constexpr int PP = 512;
constexpr int FF = 1792;
constexpr int N1 = 2 * (PP + FF);
constexpr int CHUNK = 128;
constexpr int NCHUNK = SL / CHUNK;

__device__ __forceinline__ float bf2f(u16 v) { return __uint_as_float(((unsigned)v) << 16); }
__device__ __forceinline__ u16 f2bf(float f) {
  unsigned u = __float_as_uint(f);
  u += 0x7fffu + ((u >> 16) & 1u);
  return (u16)(u >> 16);
}
__device__ __forceinline__ float rbf(float f) { return bf2f(f2bf(f)); }

constexpr size_t WS_XN   = 0;
constexpr size_t WS_W1   = WS_XN + (size_t)SL * DM * 2;
constexpr size_t WS_W2Y  = WS_W1 + (size_t)N1 * DM * 2;
constexpr size_t WS_W2D  = WS_W2Y + (size_t)DM * 2 * PP * 2;
constexpr size_t WS_PAR  = WS_W2D + (size_t)DM * FF * 2;
constexpr size_t WS_HGL  = WS_PAR + (size_t)4 * PP * 8;
constexpr size_t WS_HL   = WS_HGL + (size_t)SL * FF * 2;
constexpr size_t WS_HEND = WS_HL + (size_t)SL * 2 * PP * 4;
constexpr size_t WS_END  = WS_HEND + (size_t)NCHUNK * PP * 8;

constexpr int PB_LN = SL / 4;
constexpr int PB_TG = (DM / 64) * (FF / 64);
constexpr int PB_T0 = PB_LN;
constexpr int PB_T1 = PB_T0 + PB_TG;
constexpr int PB_T2 = PB_T1 + PB_TG;
constexpr int PB_CP = PB_T2 + PB_TG;
constexpr int PB_CY = PB_CP + 64;
constexpr int PB_PAR = PB_CY + 32;
constexpr int PB_TOTAL = PB_PAR + 1;

__global__ __launch_bounds__(256) void prep_kernel(
    const u16* __restrict__ x, const u16* __restrict__ lam_re, const u16* __restrict__ lam_im,
    const u16* __restrict__ b_re, const u16* __restrict__ b_im,
    const u16* __restrict__ c_re, const u16* __restrict__ c_im, const u16* __restrict__ log_dt,
    const u16* __restrict__ w_gate, const u16* __restrict__ w_up, const u16* __restrict__ w_down,
    char* __restrict__ ws) {
  __shared__ u16 tile[64][66];
  const int tid = threadIdx.x, b = blockIdx.x;
  if (b < PB_T0) {
    const int row = b * 4 + (tid >> 6), lane = tid & 63;
    const u16* xr = x + (size_t)row * DM;
    bf16x8 v = *(const bf16x8*)(xr + lane * 8);
    float f[8]; float s = 0.f;
#pragma unroll
    for (int j = 0; j < 8; ++j) { f[j] = bf2f((u16)v[j]); s += f[j]; }
#pragma unroll
    for (int o = 32; o > 0; o >>= 1) s += __shfl_xor(s, o);
    const float mean = s * (1.0f / DM);
    float q = 0.f;
#pragma unroll
    for (int j = 0; j < 8; ++j) { float d = f[j] - mean; q += d * d; }
#pragma unroll
    for (int o = 32; o > 0; o >>= 1) q += __shfl_xor(q, o);
    const float mb = rbf(mean);
    const float vb = rbf(q * (1.0f / DM));
    const float sd = rbf(sqrtf(rbf(vb + 1e-5f)));
    bf16x8 o;
#pragma unroll
    for (int j = 0; j < 8; ++j) o[j] = (short)f2bf(rbf(f[j] - mb) / sd);
    *(bf16x8*)((u16*)(ws + WS_XN) + (size_t)row * DM + lane * 8) = o;
  } else if (b < PB_CP) {
    int which = (b - PB_T0) / PB_TG, t = (b - PB_T0) % PB_TG;
    const u16* src; int R, C;
    if (which < 2) { src = which ? w_up : w_gate; R = DM; C = FF; }
    else { src = w_down; R = FF; C = DM; }
    const int tc_n = C / 64;
    const int r0 = (t / tc_n) * 64, c0 = (t % tc_n) * 64;
#pragma unroll
    for (int i = 0; i < 2; ++i) {
      int idx = tid + i * 256, r = idx >> 3, cc = (idx & 7) * 8;
      bf16x8 v = *(const bf16x8*)(src + (size_t)(r0 + r) * C + c0 + cc);
#pragma unroll
      for (int j = 0; j < 8; ++j) tile[r][cc + j] = (u16)v[j];
    }
    __syncthreads();
#pragma unroll
    for (int i = 0; i < 2; ++i) {
      int idx = tid + i * 256, n = idx >> 3, kk = (idx & 7) * 8;
      bf16x8 v;
#pragma unroll
      for (int j = 0; j < 8; ++j) v[j] = (short)tile[kk + j][n];
      if (which < 2) {
        int f = c0 + n;
        int pairg = (PP + f) >> 4;
        int orow = pairg * 32 + which * 16 + (f & 15);
        *(bf16x8*)((u16*)(ws + WS_W1) + (size_t)orow * DM + r0 + kk) = v;
      } else {
        *(bf16x8*)((u16*)(ws + WS_W2D) + (size_t)(c0 + n) * FF + r0 + kk) = v;
      }
    }
  } else if (b < PB_CY) {
    int blk = b - PB_CP;
    int which = blk & 1, pg = blk >> 1;
    const u16* src = which ? b_im : b_re;
    for (int idx = tid; idx < 16 * 64; idx += 256) {
      int r = idx >> 6, cc = (idx & 63) * 8;
      bf16x8 v = *(const bf16x8*)(src + (size_t)(pg * 16 + r) * DM + cc);
      *(bf16x8*)((u16*)(ws + WS_W1) + (size_t)(pg * 32 + which * 16 + r) * DM + cc) = v;
    }
  } else if (b < PB_PAR) {
    int d0 = (b - PB_CY) * 16;
    for (int idx = tid; idx < 16 * 128; idx += 256) {
      int r = idx >> 7, p0 = (idx & 127) * 4;
      const u16* cr = c_re + (size_t)(d0 + r) * PP + p0;
      const u16* ci = c_im + (size_t)(d0 + r) * PP + p0;
      bf16x8 o;
#pragma unroll
      for (int j = 0; j < 4; ++j) {
        o[2 * j] = (short)f2bf(2.f * bf2f(cr[j]));
        o[2 * j + 1] = (short)f2bf(-2.f * bf2f(ci[j]));
      }
      *(bf16x8*)((u16*)(ws + WS_W2Y) + (size_t)(d0 + r) * (2 * PP) + p0 * 2) = o;
    }
  } else {
    float2* par = (float2*)(ws + WS_PAR);
    for (int p = tid; p < PP; p += 256) {
      float lr = bf2f(lam_re[p]), li = bf2f(lam_im[p]);
      float dt = rbf(expf(bf2f(log_dt[p])));
      float zr = dt * lr, zi = dt * li;
      float e = expf(zr), sn, cs;
      sincosf(zi, &sn, &cs);
      float br = e * cs, bi = e * sn;
      float nr = br - 1.f, ni = bi;
      float den = lr * lr + li * li;
      float cr = (nr * lr + ni * li) / den, ci = (ni * lr - nr * li) / den;
      par[p] = make_float2(cr, ci);
      par[PP + p] = make_float2(br, bi);
      float e64 = expf(64.f * zr); sincosf(64.f * zi, &sn, &cs);
      par[2 * PP + p] = make_float2(e64 * cs, e64 * sn);
      float e128 = expf(128.f * zr); sincosf(128.f * zi, &sn, &cs);
      par[3 * PP + p] = make_float2(e128 * cs, e128 * sn);
    }
  }
}

constexpr int G1_BM = 128, G1_BN = 288, G1_BK = 64;
constexpr int G1_ABYTES = G1_BM * G1_BK * 2;
constexpr int G1_BBYTES = G1_BN * G1_BK * 2;
constexpr int G1_STAGE = G1_ABYTES + G1_BBYTES;
constexpr int G1_LDS = 128 * 144 * 8;

__global__ __launch_bounds__(256, 1) void gemm1_kernel(char* __restrict__ ws) {
  extern __shared__ __attribute__((aligned(16))) char smem[];
  const u16* A = (const u16*)(ws + WS_XN);
  const u16* Bt = (const u16*)(ws + WS_W1);
  const int tid = threadIdx.x, lane = tid & 63, wid = tid >> 6;
  const int fr = lane & 15, fq = lane >> 4;
  const int bid = blockIdx.x;
  const int xcd = bid & 7, loc = bid >> 3;
  const int rp = (xcd >> 1) * 4 + (loc >> 3), cp = (xcd & 1) * 8 + (loc & 7);
  const int brow = rp * G1_BM, bcol = cp * G1_BN;

  auto stage = [&](int buf, int kt) {
    char* sa = smem + buf * G1_STAGE;
    char* sb = sa + G1_ABYTES;
#pragma unroll
    for (int i = 0; i < 4; ++i) {
      int idx = tid + i * 256, r = idx >> 3, c = (idx & 7) ^ (r & 7);
      __builtin_amdgcn_global_load_lds((const unsigned*)(A + (size_t)(brow + r) * DM + kt * G1_BK + c * 8),
                                       (unsigned*)(sa + idx * 16), 16, 0, 0);
    }
#pragma unroll
    for (int i = 0; i < 9; ++i) {
      int idx = tid + i * 256, r = idx >> 3, c = (idx & 7) ^ (r & 7);
      __builtin_amdgcn_global_load_lds((const unsigned*)(Bt + (size_t)(bcol + r) * DM + kt * G1_BK + c * 8),
                                       (unsigned*)(sb + idx * 16), 16, 0, 0);
    }
  };

  f32x4 acc[2][18];
#pragma unroll
  for (int m = 0; m < 2; ++m)
#pragma unroll
    for (int n = 0; n < 18; ++n) acc[m][n] = (f32x4){0.f, 0.f, 0.f, 0.f};

  stage(0, 0);
  constexpr int NKT = DM / G1_BK;
  for (int kt = 0; kt < NKT; ++kt) {
    asm volatile("s_waitcnt vmcnt(0)" ::: "memory");
    __syncthreads();
    if (kt + 1 < NKT) stage((kt + 1) & 1, kt + 1);
    const char* sa = smem + (kt & 1) * G1_STAGE;
    const char* sb = sa + G1_ABYTES;
#pragma unroll
    for (int kk = 0; kk < 2; ++kk) {
      bf16x8 af[2], bfr[18];
#pragma unroll
      for (int m = 0; m < 2; ++m) {
        int r = wid * 32 + m * 16 + fr;
        af[m] = *(const bf16x8*)(sa + r * 128 + (((kk * 4 + fq) ^ (r & 7)) * 16));
      }
#pragma unroll
      for (int n = 0; n < 18; ++n) {
        int r = n * 16 + fr;
        bfr[n] = *(const bf16x8*)(sb + r * 128 + (((kk * 4 + fq) ^ (r & 7)) * 16));
      }
#pragma unroll
      for (int m = 0; m < 2; ++m)
#pragma unroll
        for (int n = 0; n < 18; ++n)
          acc[m][n] = __builtin_amdgcn_mfma_f32_16x16x32_bf16(af[m], bfr[n], acc[m][n], 0, 0, 0);
    }
  }
  __syncthreads();

  const float2* par = (const float2*)(ws + WS_PAR);
  u16* hgl = (u16*)(ws + WS_HGL);
  float2* sv = (float2*)smem;
  const int pg0 = cp * 9;
  bool has_ssm = (pg0 < 32);
#pragma unroll
  for (int g = 0; g < 9; ++g) {
    const int pg = pg0 + g;
    if (pg < 32) {
      const int p = pg * 16 + fr;
      const float2 c = par[p];
#pragma unroll
      for (int m = 0; m < 2; ++m)
#pragma unroll
        for (int j = 0; j < 4; ++j) {
          float re = acc[m][2 * g][j], im = acc[m][2 * g + 1][j];
          int r = wid * 32 + m * 16 + fq * 4 + j;
          sv[r * 144 + g * 16 + fr] = make_float2(c.x * re - c.y * im, c.x * im + c.y * re);
        }
    } else {
      const int f = (pg - 32) * 16 + fr;
#pragma unroll
      for (int m = 0; m < 2; ++m)
#pragma unroll
        for (int j = 0; j < 4; ++j) {
          int r = brow + wid * 32 + m * 16 + fq * 4 + j;
          float gg = rbf(acc[m][2 * g][j]), uu = rbf(acc[m][2 * g + 1][j]);
          hgl[(size_t)r * FF + f] = f2bf(gg * uu);
        }
    }
  }
  if (has_ssm) {
    __syncthreads();
    const int nss = min(9, 32 - pg0) * 16;
    if (tid < nss) {
      const int p = pg0 * 16 + tid;
      const float2 lam = par[PP + p];
      float2* hl = (float2*)(ws + WS_HL);
      float hr = 0.f, hi = 0.f;
      for (int t = 0; t < CHUNK; ++t) {
        float2 v = sv[t * 144 + tid];
        float nr = lam.x * hr - lam.y * hi + v.x;
        float ni = lam.x * hi + lam.y * hr + v.y;
        hr = nr; hi = ni;
        hl[(size_t)(brow + t) * PP + p] = make_float2(hr, hi);
      }
      ((float2*)(ws + WS_HEND))[rp * PP + p] = make_float2(hr, hi);
    }
  }
}

constexpr int G2_AY = 64 * 1024 * 2;
constexpr int G2_T = 64 * 64 * 2;
constexpr int G2_LDS = G2_AY + 4 * G2_T;

__global__ __launch_bounds__(256, 1) void gemm2_kernel(char* __restrict__ ws, const u16* __restrict__ x,
                                                     const u16* __restrict__ dskip, float* __restrict__ out) {
  extern __shared__ __attribute__((aligned(16))) char smem[];
  const int tid = threadIdx.x, lane = tid & 63, wid = tid >> 6;
  const int fr = lane & 15, fq = lane >> 4, wr = wid >> 1, wc = wid & 1;
  const int bid = blockIdx.x;
  const int xcd = bid & 7, loc = bid >> 3;
  const int rt = xcd * 4 + (loc >> 3), ct = loc & 7;
  const int brow = rt * 64, bcol = ct * 64;
  const float2* par = (const float2*)(ws + WS_PAR);
  const u16* W2Y = (const u16*)(ws + WS_W2Y);
  const u16* W2D = (const u16*)(ws + WS_W2D);
  const u16* HGL = (const u16*)(ws + WS_HGL);
  char* sT = smem + G2_AY;

  auto stage_tile = [&](char* dst, const u16* src, int ld, int k0) {
#pragma unroll
    for (int i = 0; i < 2; ++i) {
      int idx = tid + i * 256, r = idx >> 3, c = (idx & 7) ^ (r & 7);
      __builtin_amdgcn_global_load_lds((const unsigned*)(src + (size_t)r * ld + k0 + c * 8),
                                       (unsigned*)(dst + idx * 16), 16, 0, 0);
    }
  };
  stage_tile(sT, W2Y + (size_t)bcol * 1024, 1024, 0);

  {
    const int chunk = brow / CHUNK, half = (brow / 64) & 1;
    const float2* hl = (const float2*)(ws + WS_HL);
    const float2* hend = (const float2*)(ws + WS_HEND);
#pragma unroll
    for (int q = 0; q < 2; ++q) {
      const int p = tid + q * 256;
      const float2 lam = par[PP + p], l128 = par[3 * PP + p];
      float sr = 0.f, si = 0.f;
      for (int j = 0; j < chunk; ++j) {
        float2 e = hend[j * PP + p];
        float nr = l128.x * sr - l128.y * si + e.x;
        float ni = l128.x * si + l128.y * sr + e.y;
        sr = nr; si = ni;
      }
      float zr = lam.x * sr - lam.y * si, zi = lam.x * si + lam.y * sr;
      if (half) {
        const float2 l64 = par[2 * PP + p];
        float tr = l64.x * zr - l64.y * zi, ti = l64.x * zi + l64.y * zr;
        zr = tr; zi = ti;
      }
      const int cchunk = p >> 2, coff = (p & 3) * 4;
      for (int r = 0; r < 64; ++r) {
        float2 h = hl[(size_t)(brow + r) * PP + p];
        unsigned pk = (unsigned)f2bf(h.x + zr) | ((unsigned)f2bf(h.y + zi) << 16);
        *(unsigned*)(smem + r * 2048 + ((cchunk ^ (r & 7)) * 16) + coff) = pk;
        float tr = lam.x * zr - lam.y * zi, ti = lam.x * zi + lam.y * zr;
        zr = tr; zi = ti;
      }
    }
  }

  f32x4 accy[2][2], accd[2][2];
#pragma unroll
  for (int m = 0; m < 2; ++m)
#pragma unroll
    for (int n = 0; n < 2; ++n) { accy[m][n] = (f32x4){0, 0, 0, 0}; accd[m][n] = (f32x4){0, 0, 0, 0}; }

  for (int kt = 0; kt < 16; ++kt) {
    asm volatile("s_waitcnt vmcnt(0)" ::: "memory");
    __syncthreads();
    if (kt + 1 < 16) stage_tile(sT + ((kt + 1) & 1) * G2_T, W2Y + (size_t)bcol * 1024, 1024, (kt + 1) * 64);
    else { stage_tile(sT + 2 * G2_T, HGL + (size_t)brow * FF, FF, 0); stage_tile(sT + 3 * G2_T, W2D + (size_t)bcol * FF, FF, 0); }
    const char* sb = sT + (kt & 1) * G2_T;
#pragma unroll
    for (int kk = 0; kk < 2; ++kk) {
      bf16x8 af[2], bfr[2];
#pragma unroll
      for (int m = 0; m < 2; ++m) {
        int r = wr * 32 + m * 16 + fr;
        int c = kt * 8 + kk * 4 + fq;
        af[m] = *(const bf16x8*)(smem + r * 2048 + ((c ^ (r & 7)) * 16));
      }
#pragma unroll
      for (int n = 0; n < 2; ++n) {
        int r = wc * 32 + n * 16 + fr;
        bfr[n] = *(const bf16x8*)(sb + r * 128 + (((kk * 4 + fq) ^ (r & 7)) * 16));
      }
#pragma unroll
      for (int m = 0; m < 2; ++m)
#pragma unroll
        for (int n = 0; n < 2; ++n)
          accy[m][n] = __builtin_amdgcn_mfma_f32_16x16x32_bf16(af[m], bfr[n], accy[m][n], 0, 0, 0);
    }
  }
  char* bufA[2] = {sT + 2 * G2_T, smem};
  char* bufB[2] = {sT + 3 * G2_T, smem + G2_T};
  for (int kt = 0; kt < 28; ++kt) {
    asm volatile("s_waitcnt vmcnt(0)" ::: "memory");
    __syncthreads();
    if (kt + 1 < 28) {
      stage_tile(bufA[(kt + 1) & 1], HGL + (size_t)brow * FF, FF, (kt + 1) * 64);
      stage_tile(bufB[(kt + 1) & 1], W2D + (size_t)bcol * FF, FF, (kt + 1) * 64);
    }
    const char* sa = bufA[kt & 1];
    const char* sb = bufB[kt & 1];
#pragma unroll
    for (int kk = 0; kk < 2; ++kk) {
      bf16x8 af[2], bfr[2];
#pragma unroll
      for (int m = 0; m < 2; ++m) {
        int r = wr * 32 + m * 16 + fr;
        af[m] = *(const bf16x8*)(sa + r * 128 + (((kk * 4 + fq) ^ (r & 7)) * 16));
      }
#pragma unroll
      for (int n = 0; n < 2; ++n) {
        int r = wc * 32 + n * 16 + fr;
        bfr[n] = *(const bf16x8*)(sb + r * 128 + (((kk * 4 + fq) ^ (r & 7)) * 16));
      }
#pragma unroll
      for (int m = 0; m < 2; ++m)
#pragma unroll
        for (int n = 0; n < 2; ++n)
          accd[m][n] = __builtin_amdgcn_mfma_f32_16x16x32_bf16(af[m], bfr[n], accd[m][n], 0, 0, 0);
    }
  }
  const u16* xn = (const u16*)(ws + WS_XN);
#pragma unroll
  for (int m = 0; m < 2; ++m)
#pragma unroll
    for (int n = 0; n < 2; ++n) {
      const int d = bcol + wc * 32 + n * 16 + fr;
      const float ds = bf2f(dskip[d]);
#pragma unroll
      for (int j = 0; j < 4; ++j) {
        const int t = brow + wr * 32 + m * 16 + fq * 4 + j;
        const size_t o = (size_t)t * DM + d;
        float s = accy[m][n][j] + rbf(bf2f(xn[o]) * ds);
        float dn = rbf(accd[m][n][j]);
        float g = rbf(dn / (1.f + __expf(-dn)));
        out[o] = bf2f(x[o]) + s * g;
      }
    }
}

extern "C" void kernel_launch(void* const* d_in, const int* in_sizes, int n_in,
                              void* d_out, int out_size, void* d_ws, size_t ws_size,
                              hipStream_t stream) {
  const u16* x = (const u16*)d_in[0];
  const u16* lam_re = (const u16*)d_in[1];
  const u16* lam_im = (const u16*)d_in[2];
  const u16* b_re = (const u16*)d_in[3];
  const u16* b_im = (const u16*)d_in[4];
  const u16* c_re = (const u16*)d_in[5];
  const u16* c_im = (const u16*)d_in[6];
  const u16* log_dt = (const u16*)d_in[7];
  const u16* dskip = (const u16*)d_in[8];
  const u16* w_gate = (const u16*)d_in[10];
  const u16* w_up = (const u16*)d_in[11];
  const u16* w_down = (const u16*)d_in[12];
  char* ws = (char*)d_ws;
  hipFuncSetAttribute((const void*)gemm1_kernel, hipFuncAttributeMaxDynamicSharedMemorySize, G1_LDS);
  hipFuncSetAttribute((const void*)gemm2_kernel, hipFuncAttributeMaxDynamicSharedMemorySize, G2_LDS);
  prep_kernel<<<PB_TOTAL, 256, 0, stream>>>(x, lam_re, lam_im, b_re, b_im, c_re, c_im, log_dt, w_gate, w_up, w_down, ws);
  gemm1_kernel<<<256, 256, G1_LDS, stream>>>(ws);
  gemm2_kernel<<<256, 256, G2_LDS, stream>>>(ws, x, dskip, (float*)d_out);
}
```

```cpp
#include <hip/hip_runtime.h>
#include <hip/hip_bf16.h>
#include <stdint.h>

typedef __attribute__((ext_vector_type(8))) short bf16x8;
typedef __attribute__((ext_vector_type(4))) float f32x4;
typedef unsigned short u16;

constexpr int SL = 2048;
constexpr int DM = 512;
constexpr int PP = 512;
constexpr int FF = 1792;
constexpr int N1 = 2 * (PP + FF);
constexpr int CHUNK = 128;
constexpr int NCHUNK = SL / CHUNK;

__device__ __forceinline__ float bf2f(u16 v) { return __uint_as_float(((unsigned)v) << 16); }
__device__ __forceinline__ u16 f2bf(float f) {
  unsigned u = __float_as_uint(f);
  u += 0x7fffu + ((u >> 16) & 1u);
  return (u16)(u >> 16);
}
__device__ __forceinline__ float rbf(float f) { return bf2f(f2bf(f)); }

constexpr size_t WS_XN   = 0;
constexpr size_t WS_W1   = WS_XN + (size_t)SL * DM * 2;
constexpr size_t WS_W2Y  = WS_W1 + (size_t)N1 * DM * 2;
constexpr size_t WS_W2D  = WS_W2Y + (size_t)DM * 2 * PP * 2;
constexpr size_t WS_PAR  = WS_W2D + (size_t)DM * FF * 2;
constexpr size_t WS_HGL  = WS_PAR + (size_t)4 * PP * 8;
constexpr size_t WS_HL   = WS_HGL + (size_t)SL * FF * 2;
constexpr size_t WS_HEND = WS_HL + (size_t)SL * 2 * PP * 4;
constexpr size_t WS_END  = WS_HEND + (size_t)NCHUNK * PP * 8;

constexpr int PB_LN = SL / 4;
constexpr int PB_TG = (DM / 64) * (FF / 64);
constexpr int PB_T0 = PB_LN;
constexpr int PB_T1 = PB_T0 + PB_TG;
constexpr int PB_T2 = PB_T1 + PB_TG;
constexpr int PB_CP = PB_T2 + PB_TG;
constexpr int PB_CY = PB_CP + 64;
constexpr int PB_PAR = PB_CY + 32;
constexpr int PB_TOTAL = PB_PAR + 1;

__global__ __launch_bounds__(256) void prep_kernel(
    const u16* __restrict__ x, const u16* __restrict__ lam_re, const u16* __restrict__ lam_im,
    const u16* __restrict__ b_re, const u16* __restrict__ b_im,
    const u16* __restrict__ c_re, const u16* __restrict__ c_im, const u16* __restrict__ log_dt,
    const u16* __restrict__ w_gate, const u16* __restrict__ w_up, const u16* __restrict__ w_down,
    char* __restrict__ ws) {
  __shared__ u16 tile[64][66];
  const int tid = threadIdx.x, b = blockIdx.x;
  if (b < PB_T0) {
    const int row = b * 4 + (tid >> 6), lane = tid & 63;
    const u16* xr = x + (size_t)row * DM;
    bf16x8 v = *(const bf16x8*)(xr + lane * 8);
    float f[8]; float s = 0.f;
#pragma unroll
    for (int j = 0; j < 8; ++j) { f[j] = bf2f((u16)v[j]); s += f[j]; }
#pragma unroll
    for (int o = 32; o > 0; o >>= 1) s += __shfl_xor(s, o);
    const float mean = s * (1.0f / DM);
    float q = 0.f;
#pragma unroll
    for (int j = 0; j < 8; ++j) { float d = f[j] - mean; q += d * d; }
#pragma unroll
    for (int o = 32; o > 0; o >>= 1) q += __shfl_xor(q, o);
    const float mb = rbf(mean);
    const float vb = rbf(q * (1.0f / DM));
    const float sd = rbf(sqrtf(rbf(vb + 1e-5f)));
    bf16x8 o;
#pragma unroll
    for (int j = 0; j < 8; ++j) o[j] = (short)f2bf(rbf(f[j] - mb) / sd);
    *(bf16x8*)((u16*)(ws + WS_XN) + (size_t)row * DM + lane * 8) = o;
  } else if (b < PB_CP) {
    int which = (b - PB_T0) / PB_TG, t = (b - PB_T0) % PB_TG;
    const u16* src; int R, C;
    if (which < 2) { src = which ? w_up : w_gate; R = DM; C = FF; }
    else { src = w_down; R = FF; C = DM; }
    const int tc_n = C / 64;
    const int r0 = (t / tc_n) * 64, c0 = (t % tc_n) * 64;
#pragma unroll
    for (int i = 0; i < 2; ++i) {
      int idx = tid + i * 256, r = idx >> 3, cc = (idx & 7) * 8;
      bf16x8 v = *(const bf16x8*)(src + (size_t)(r0 + r) * C + c0 + cc);
#pragma unroll
      for (int j = 0; j < 8; ++j) tile[r][cc + j] = (u16)v[j];
    }
    __syncthreads();
#pragma unroll
    for (int i = 0; i < 2; ++i) {
      int idx = tid + i * 256, n = idx >> 3, kk = (idx & 7) * 8;
      bf16x8 v;
#pragma unroll
      for (int j = 0; j < 8; ++j) v[j] = (short)tile[kk + j][n];
      if (which < 2) {
        int f = c0 + n;
        int pairg = (PP + f) >> 4;
        int orow = pairg * 32 + which * 16 + (f & 15);
        *(bf16x8*)((u16*)(ws + WS_W1) + (size_t)orow * DM + r0 + kk) = v;
      } else {
        *(bf16x8*)((u16*)(ws + WS_W2D) + (size_t)(c0 + n) * FF + r0 + kk) = v;
      }
    }
  } else if (b < PB_CY) {
    int blk = b - PB_CP;
    int which = blk & 1, pg = blk >> 1;
    const u16* src = which ? b_im : b_re;
    for (int idx = tid; idx < 16 * 64; idx += 256) {
      int r = idx >> 6, cc = (idx & 63) * 8;
      bf16x8 v = *(const bf16x8*)(src + (size_t)(pg * 16 + r) * DM + cc);
      *(bf16x8*)((u16*)(ws + WS_W1) + (size_t)(pg * 32 + which * 16 + r) * DM + cc) = v;
    }
  } else if (b < PB_PAR) {
    int d0 = (b - PB_CY) * 16;
    for (int idx = tid; idx < 16 * 128; idx += 256) {
      int r = idx >> 7, p0 = (idx & 127) * 4;
      const u16* cr = c_re + (size_t)(d0 + r) * PP + p0;
      const u16* ci = c_im + (size_t)(d0 + r) * PP + p0;
      bf16x8 o;
#pragma unroll
      for (int j = 0; j < 4; ++j) {
        o[2 * j] = (short)f2bf(2.f * bf2f(cr[j]));
        o[2 * j + 1] = (short)f2bf(-2.f * bf2f(ci[j]));
      }
      *(bf16x8*)((u16*)(ws + WS_W2Y) + (size_t)(d0 + r) * (2 * PP) + p0 * 2) = o;
    }
  } else {
    float2* par = (float2*)(ws + WS_PAR);
    for (int p = tid; p < PP; p += 256) {
      float lr = bf2f(lam_re[p]), li = bf2f(lam_im[p]);
      float dt = rbf(expf(bf2f(log_dt[p])));
      float zr = dt * lr, zi = dt * li;
      float e = expf(zr), sn, cs;
      sincosf(zi, &sn, &cs);
      float br = e * cs, bi = e * sn;
      float nr = br - 1.f, ni = bi;
      float den = lr * lr + li * li;
      float cr = (nr * lr + ni * li) / den, ci = (ni * lr - nr * li) / den;
      par[p] = make_float2(cr, ci);
      par[PP + p] = make_float2(br, bi);
      float e64 = expf(64.f * zr); sincosf(64.f * zi, &sn, &cs);
      par[2 * PP + p] = make_float2(e64 * cs, e64 * sn);
      float e128 = expf(128.f * zr); sincosf(128.f * zi, &sn, &cs);
      par[3 * PP + p] = make_float2(e128 * cs, e128 * sn);
    }
  }
}

constexpr int G1_BM = 128, G1_BN = 288, G1_BK = 64;
constexpr int G1_ABYTES = G1_BM * G1_BK * 2;
constexpr int G1_BBYTES = G1_BN * G1_BK * 2;
constexpr int G1_STAGE = G1_ABYTES + G1_BBYTES;
constexpr int G1_LDS = 3 * G1_STAGE;
#define WAIT_VM(n) asm volatile("s_waitcnt vmcnt(" #n ")" ::: "memory")

__global__ __launch_bounds__(256, 1) void gemm1_kernel(char* __restrict__ ws) {
  extern __shared__ __attribute__((aligned(16))) char smem[];
  const u16* A = (const u16*)(ws + WS_XN);
  const u16* Bt = (const u16*)(ws + WS_W1);
  const int tid = threadIdx.x, lane = tid & 63, wid = tid >> 6;
  const int fr = lane & 15, fq = lane >> 4;
  const int bid = blockIdx.x;
  const int xcd = bid & 7, loc = bid >> 3;
  const int rp = (xcd >> 1) * 4 + (loc >> 3), cp = (xcd & 1) * 8 + (loc & 7);
  const int brow = rp * G1_BM, bcol = cp * G1_BN;

  auto stage = [&](int buf, int kt) {
    char* sa = smem + buf * G1_STAGE;
    char* sb = sa + G1_ABYTES;
#pragma unroll
    for (int i = 0; i < 4; ++i) {
      int idx = tid + i * 256, r = idx >> 3, c = (idx & 7) ^ (r & 7);
      __builtin_amdgcn_global_load_lds((const unsigned*)(A + (size_t)(brow + r) * DM + kt * G1_BK + c * 8),
                                       (unsigned*)(sa + idx * 16), 16, 0, 0);
    }
#pragma unroll
    for (int i = 0; i < 9; ++i) {
      int idx = tid + i * 256, r = idx >> 3, c = (idx & 7) ^ (r & 7);
      __builtin_amdgcn_global_load_lds((const unsigned*)(Bt + (size_t)(bcol + r) * DM + kt * G1_BK + c * 8),
                                       (unsigned*)(sb + idx * 16), 16, 0, 0);
    }
  };

  f32x4 acc[2][18];
#pragma unroll
  for (int m = 0; m < 2; ++m)
#pragma unroll
    for (int n = 0; n < 18; ++n) acc[m][n] = (f32x4){0.f, 0.f, 0.f, 0.f};

  stage(0, 0);
  stage(1, 1);
  constexpr int NKT = DM / G1_BK;
#pragma unroll
  for (int kt = 0; kt < NKT; ++kt) {
    if (kt + 1 < NKT) WAIT_VM(13); else WAIT_VM(0);
    __builtin_amdgcn_s_barrier();
    if (kt + 2 < NKT) stage((kt + 2) % 3, kt + 2);
    const char* sa = smem + (kt % 3) * G1_STAGE;
    const char* sb = sa + G1_ABYTES;
#pragma unroll
    for (int kk = 0; kk < 2; ++kk) {
      bf16x8 af[2], bfr[18];
#pragma unroll
      for (int m = 0; m < 2; ++m) {
        int r = wid * 32 + m * 16 + fr;
        af[m] = *(const bf16x8*)(sa + r * 128 + (((kk * 4 + fq) ^ (r & 7)) * 16));
      }
#pragma unroll
      for (int n = 0; n < 18; ++n) {
        int r = n * 16 + fr;
        bfr[n] = *(const bf16x8*)(sb + r * 128 + (((kk * 4 + fq) ^ (r & 7)) * 16));
      }
#pragma unroll
      for (int m = 0; m < 2; ++m)
#pragma unroll
        for (int n = 0; n < 18; ++n)
          acc[m][n] = __builtin_amdgcn_mfma_f32_16x16x32_bf16(af[m], bfr[n], acc[m][n], 0, 0, 0);
    }
  }
  __syncthreads();

  const float2* par = (const float2*)(ws + WS_PAR);
  u16* hgl = (u16*)(ws + WS_HGL);
  float2* sv = (float2*)smem;
  const int pg0 = cp * 9;
  bool has_ssm = (pg0 < 32);
#pragma unroll
  for (int g = 0; g < 9; ++g) {
    const int pg = pg0 + g;
    if (pg < 32) {
      const int p = pg * 16 + fr;
      const float2 c = par[p];
#pragma unroll
      for (int m = 0; m < 2; ++m)
#pragma unroll
        for (int j = 0; j < 4; ++j) {
          float re = acc[m][2 * g][j], im = acc[m][2 * g + 1][j];
          int r = wid * 32 + m * 16 + fq * 4 + j;
          sv[r * 144 + g * 16 + fr] = make_float2(c.x * re - c.y * im, c.x * im + c.y * re);
        }
    } else {
      const int f = (pg - 32) * 16 + fr;
#pragma unroll
      for (int m = 0; m < 2; ++m)
#pragma unroll
        for (int j = 0; j < 4; ++j) {
          int r = brow + wid * 32 + m * 16 + fq * 4 + j;
          float gg = rbf(acc[m][2 * g][j]), uu = rbf(acc[m][2 * g + 1][j]);
          hgl[(size_t)r * FF + f] = f2bf(gg * uu);
        }
    }
  }
  if (has_ssm) {
    __syncthreads();
    const int nss = min(9, 32 - pg0) * 16;
    if (tid < nss) {
      const int p = pg0 * 16 + tid;
      const float2 lam = par[PP + p];
      float2* hl = (float2*)(ws + WS_HL);
      float hr = 0.f, hi = 0.f;
      for (int t = 0; t < CHUNK; ++t) {
        float2 v = sv[t * 144 + tid];
        float nr = lam.x * hr - lam.y * hi + v.x;
        float ni = lam.x * hi + lam.y * hr + v.y;
        hr = nr; hi = ni;
        hl[(size_t)(brow + t) * PP + p] = make_float2(hr, hi);
      }
      ((float2*)(ws + WS_HEND))[rp * PP + p] = make_float2(hr, hi);
    }
  }
}

constexpr int G2_AY = 64 * 1024 * 2;
constexpr int G2_T = 64 * 64 * 2;
constexpr int G2_LDS = G2_AY + 4 * G2_T;

__global__ __launch_bounds__(256, 1) void gemm2_kernel(char* __restrict__ ws, const u16* __restrict__ x,
                                                     const u16* __restrict__ dskip, float* __restrict__ out) {
  extern __shared__ __attribute__((aligned(16))) char smem[];
  const int tid = threadIdx.x, lane = tid & 63, wid = tid >> 6;
  const int fr = lane & 15, fq = lane >> 4, wr = wid >> 1, wc = wid & 1;
  const int bid = blockIdx.x;
  const int xcd = bid & 7, loc = bid >> 3;
  const int rt = xcd * 4 + (loc >> 3), ct = loc & 7;
  const int brow = rt * 64, bcol = ct * 64;
  const float2* par = (const float2*)(ws + WS_PAR);
  const u16* W2Y = (const u16*)(ws + WS_W2Y);
  const u16* W2D = (const u16*)(ws + WS_W2D);
  const u16* HGL = (const u16*)(ws + WS_HGL);
  char* sT = smem + G2_AY;

  auto stage_tile = [&](char* dst, const u16* src, int ld, int k0) {
#pragma unroll
    for (int i = 0; i < 2; ++i) {
      int idx = tid + i * 256, r = idx >> 3, c = (idx & 7) ^ (r & 7);
      __builtin_amdgcn_global_load_lds((const unsigned*)(src + (size_t)r * ld + k0 + c * 8),
                                       (unsigned*)(dst + idx * 16), 16, 0, 0);
    }
  };
#pragma unroll
  for (int i = 0; i < 3; ++i) stage_tile(sT + i * G2_T, W2Y + (size_t)bcol * 1024, 1024, i * 64);

  {
    const int chunk = brow / CHUNK, half = (brow / 64) & 1;
    const float2* hl = (const float2*)(ws + WS_HL);
    const float2* hend = (const float2*)(ws + WS_HEND);
#pragma unroll
    for (int q = 0; q < 2; ++q) {
      const int p = tid + q * 256;
      const float2 lam = par[PP + p], l128 = par[3 * PP + p];
      float sr = 0.f, si = 0.f;
      for (int j = 0; j < chunk; ++j) {
        float2 e = hend[j * PP + p];
        float nr = l128.x * sr - l128.y * si + e.x;
        float ni = l128.x * si + l128.y * sr + e.y;
        sr = nr; si = ni;
      }
      float zr = lam.x * sr - lam.y * si, zi = lam.x * si + lam.y * sr;
      if (half) {
        const float2 l64 = par[2 * PP + p];
        float tr = l64.x * zr - l64.y * zi, ti = l64.x * zi + l64.y * zr;
        zr = tr; zi = ti;
      }
      const int cchunk = p >> 2, coff = (p & 3) * 4;
#pragma unroll 16
      for (int r = 0; r < 64; ++r) {
        float2 h = hl[(size_t)(brow + r) * PP + p];
        unsigned pk = (unsigned)f2bf(h.x + zr) | ((unsigned)f2bf(h.y + zi) << 16);
        *(unsigned*)(smem + r * 2048 + ((cchunk ^ (r & 7)) * 16) + coff) = pk;
        float tr = lam.x * zr - lam.y * zi, ti = lam.x * zi + lam.y * zr;
        zr = tr; zi = ti;
      }
    }
  }

  f32x4 accy[2][2], accd[2][2];
#pragma unroll
  for (int m = 0; m < 2; ++m)
#pragma unroll
    for (int n = 0; n < 2; ++n) { accy[m][n] = (f32x4){0, 0, 0, 0}; accd[m][n] = (f32x4){0, 0, 0, 0}; }

  asm volatile("s_waitcnt lgkmcnt(0)" ::: "memory");
#pragma unroll
  for (int kt = 0; kt < 16; ++kt) {
    if (kt + 2 < 16) WAIT_VM(4); else if (kt + 1 < 16) WAIT_VM(2); else WAIT_VM(0);
    __builtin_amdgcn_s_barrier();
    if (kt + 3 < 16) stage_tile(sT + ((kt + 3) & 3) * G2_T, W2Y + (size_t)bcol * 1024, 1024, (kt + 3) * 64);
    const char* sb = sT + (kt & 3) * G2_T;
#pragma unroll
    for (int kk = 0; kk < 2; ++kk) {
      bf16x8 af[2], bfr[2];
#pragma unroll
      for (int m = 0; m < 2; ++m) {
        int r = wr * 32 + m * 16 + fr;
        int c = kt * 8 + kk * 4 + fq;
        af[m] = *(const bf16x8*)(smem + r * 2048 + ((c ^ (r & 7)) * 16));
      }
#pragma unroll
      for (int n = 0; n < 2; ++n) {
        int r = wc * 32 + n * 16 + fr;
        bfr[n] = *(const bf16x8*)(sb + r * 128 + (((kk * 4 + fq) ^ (r & 7)) * 16));
      }
#pragma unroll
      for (int m = 0; m < 2; ++m)
#pragma unroll
        for (int n = 0; n < 2; ++n)
          accy[m][n] = __builtin_amdgcn_mfma_f32_16x16x32_bf16(af[m], bfr[n], accy[m][n], 0, 0, 0);
    }
  }
  __builtin_amdgcn_s_barrier();
#pragma unroll
  for (int i = 0; i < 7; ++i) {
    stage_tile(smem + i * 2 * G2_T, HGL + (size_t)brow * FF, FF, i * 64);
    stage_tile(smem + i * 2 * G2_T + G2_T, W2D + (size_t)bcol * FF, FF, i * 64);
  }
#pragma unroll
  for (int kt = 0; kt < 28; ++kt) {
    if (kt + 6 < 28) WAIT_VM(24); else if (kt + 5 < 28) WAIT_VM(20); else if (kt + 4 < 28) WAIT_VM(16);
    else if (kt + 3 < 28) WAIT_VM(12); else if (kt + 2 < 28) WAIT_VM(8); else if (kt + 1 < 28) WAIT_VM(4); else WAIT_VM(0);
    __builtin_amdgcn_s_barrier();
    if (kt + 7 < 28) {
      stage_tile(smem + ((kt + 7) & 7) * 2 * G2_T, HGL + (size_t)brow * FF, FF, (kt + 7) * 64);
      stage_tile(smem + ((kt + 7) & 7) * 2 * G2_T + G2_T, W2D + (size_t)bcol * FF, FF, (kt + 7) * 64);
    }
    const char* sa = smem + (kt & 7) * 2 * G2_T;
    const char* sb = sa + G2_T;
#pragma unroll
    for (int kk = 0; kk < 2; ++kk) {
      bf16x8 af[2], bfr[2];
#pragma unroll
      for (int m = 0; m < 2; ++m) {
        int r = wr * 32 + m * 16 + fr;
        af[m] = *(const bf16x8*)(sa + r * 128 + (((kk * 4 + fq) ^ (r & 7)) * 16));
      }
#pragma unroll
      for (int n = 0; n < 2; ++n) {
        int r = wc * 32 + n * 16 + fr;
        bfr[n] = *(const bf16x8*)(sb + r * 128 + (((kk * 4 + fq) ^ (r & 7)) * 16));
      }
#pragma unroll
      for (int m = 0; m < 2; ++m)
#pragma unroll
        for (int n = 0; n < 2; ++n)
          accd[m][n] = __builtin_amdgcn_mfma_f32_16x16x32_bf16(af[m], bfr[n], accd[m][n], 0, 0, 0);
    }
  }
  const u16* xn = (const u16*)(ws + WS_XN);
#pragma unroll
  for (int m = 0; m < 2; ++m)
#pragma unroll
    for (int n = 0; n < 2; ++n) {
      const int d = bcol + wc * 32 + n * 16 + fr;
      const float ds = bf2f(dskip[d]);
#pragma unroll
      for (int j = 0; j < 4; ++j) {
        const int t = brow + wr * 32 + m * 16 + fq * 4 + j;
        const size_t o = (size_t)t * DM + d;
        float s = accy[m][n][j] + rbf(bf2f(xn[o]) * ds);
        float dn = rbf(accd[m][n][j]);
        float g = rbf(dn / (1.f + __expf(-dn)));
        out[o] = bf2f(x[o]) + s * g;
      }
    }
}

extern "C" void kernel_launch(void* const* d_in, const int* in_sizes, int n_in,
                              void* d_out, int out_size, void* d_ws, size_t ws_size,
                              hipStream_t stream) {
  const u16* x = (const u16*)d_in[0];
  const u16* lam_re = (const u16*)d_in[1];
  const u16* lam_im = (const u16*)d_in[2];
  const u16* b_re = (const u16*)d_in[3];
  const u16* b_im = (const u16*)d_in[4];
  const u16* c_re = (const u16*)d_in[5];
  const u16* c_im = (const u16*)d_in[6];
  const u16* log_dt = (const u16*)d_in[7];
  const u16* dskip = (const u16*)d_in[8];
  const u16* w_gate = (const u16*)d_in[10];
  const u16* w_up = (const u16*)d_in[11];
  const u16* w_down = (const u16*)d_in[12];
  char* ws = (char*)d_ws;
  hipFuncSetAttribute((const void*)gemm1_kernel, hipFuncAttributeMaxDynamicSharedMemorySize, G1_LDS);
  hipFuncSetAttribute((const void*)gemm2_kernel, hipFuncAttributeMaxDynamicSharedMemorySize, G2_LDS);
  prep_kernel<<<PB_TOTAL, 256, 0, stream>>>(x, lam_re, lam_im, b_re, b_im, c_re, c_im, log_dt, w_gate, w_up, w_down, ws);
  gemm1_kernel<<<256, 256, G1_LDS, stream>>>(ws);
  gemm2_kernel<<<256, 256, G2_LDS, stream>>>(ws, x, dskip, (float*)d_out);
}
```
